```python
import torch

def _softmax(x, axis=-1):
    return torch.softmax(x, dim=axis)

def _gelu(x):
    return torch.nn.functional.gelu(x, approximate='tanh')

def _silu(x):
    return torch.nn.functional.silu(x)

def _sigmoid(x):
    return torch.sigmoid(x)

def _layernorm(x, axis=-1, eps=1e-05):
    m = x.mean(dim=axis, keepdim=True)
    v = x.var(dim=axis, keepdim=True, unbiased=False)
    return (x - m) / torch.sqrt(v + eps)

def _scatter_add(tgt, idx, src, axis=0):
    return tgt.clone().index_add_(axis, idx, src)

def _segment_sum(src, seg, n):
    out = torch.zeros((n,) + src.shape[1:], dtype=src.dtype)
    return out.index_add_(0, seg, src)

def _pad(x, pw):
    flat = tuple((v for pair in reversed(pw) for v in pair))
    return torch.nn.functional.pad(x, flat)
kernel_name = 'standalone__autoregressive_conv_wavernn__speech'

def setup_inputs():

    _w = lambda a: (lambda t: t.to(torch.bfloat16) if t.is_floating_point() else t)(torch.as_tensor(a))
    B, T, D, K = (2, 1000, 512, 5)
    return {'x': _w((lambda *s: torch.randn(*s, dtype=torch.bfloat16) * 0.05)(B, T, D)), 'w': _w((lambda *s: torch.randn(*s, dtype=torch.bfloat16) * 0.05)(K, D, 2 * D))}

def reference(x, w):
    B, T, D = x.shape
    K = w.shape[0]
    dil = 2
    pad = (K - 1) * dil
    xp = _pad(x, ((0, 0), (pad, 0), (0, 0)))
    h = torch.einsum('btd,de->bte', xp[:, 0:T, :], w[0])
    for i in range(1, K):
        h = h + torch.einsum('btd,de->bte', xp[:, i * dil:i * dil + T, :], w[i])
    f, g = (h[..., :D].clone(), h[..., D:].clone())
    return torch.tanh(f) * _sigmoid(g)
```

```cpp
#include <hip/hip_runtime.h>
#include <hip/hip_bf16.h>
#include <stdint.h>
#include <utility>

typedef float    f32x16 __attribute__((ext_vector_type(16)));
typedef float    f32x4  __attribute__((ext_vector_type(4)));
typedef __bf16   bf16x8 __attribute__((ext_vector_type(8)));
typedef unsigned u32x2  __attribute__((ext_vector_type(2)));
typedef unsigned u32x4  __attribute__((ext_vector_type(4)));

#define DI __device__ __forceinline__
#define GAP() __builtin_amdgcn_sched_barrier(0)

namespace {

constexpr int TLEN   = 1000;
constexpr int NSTEP  = 40;
constexpr int XB     = 4;
constexpr int RS     = 10;
constexpr int XROWS  = 136;
constexpr int XSLAB  = XROWS * 128;
constexpr int WSLOT  = 2048;
constexpr int WRING  = RS * WSLOT;
constexpr int LDS_X  = 0;
constexpr int LDS_W  = XB * XSLAB;
constexpr int LDS_B  = LDS_W + 4 * WRING;
constexpr int RED_B  = 4 * 4 * 2 * 4096;
static_assert(RED_B <= LDS_B, "reduction image must fit");

constexpr int NPRO = XB + RS;
constexpr int PRO_KIND[NPRO] = {0, 1, 1, 0, 1, 1, 1, 0, 1, 1, 1, 0, 1, 1};
constexpr int PRO_IDX[NPRO]  = {0, 0, 1, 1, 2, 3, 4, 2, 5, 6, 7, 3, 8, 9};

struct Sched {
    int pro_n;
    int wait_n[NSTEP];
    constexpr Sched() : pro_n(0), wait_n() {
        int pos = 0;
        int xdone[8] = {0, 0, 0, 0, 0, 0, 0, 0};
        int wdone[NSTEP] = {};
        for (int p = 0; p < NPRO; ++p) {
            if (PRO_KIND[p] == 0) { pos += 5; xdone[PRO_IDX[p]] = pos; }
            else                  { pos += 2; wdone[PRO_IDX[p]] = pos; }
        }
        { int need = xdone[0] > wdone[0] ? xdone[0] : wdone[0]; pro_n = pos - need; }
        for (int u = 0; u < NSTEP; ++u) {
            int need = 0;
            if (u + 1 < NSTEP) {
                need = wdone[u + 1];
                if (u % 5 == 4 && xdone[(u + 1) / 5] > need) need = xdone[(u + 1) / 5];
            }
            int n = pos - need;
            wait_n[u] = n > 63 ? 63 : n;
            if (u + RS < NSTEP) { pos += 2; wdone[u + RS] = pos; }
            if (u >= 4) { int c2 = (u - 4) / 5 + XB; if (c2 < 8) { pos += 1; xdone[c2] = pos; } }
        }
    }
};
constexpr Sched SCHED{};

struct Ctx {
    f32x16 acc[4][2];
    bf16x8 A[2][4];
    u32x2  Bh[2][2][2];
    unsigned aaddr[5];
    unsigned baddr;
    unsigned xvoff[5];
    unsigned wvoff;
    unsigned xm0, wm0;
    __amdgpu_buffer_rsrc_t xsrd, wsrd;
};

template <int C, int J> DI void dma_x(Ctx& c) {
    const unsigned m0 = c.xm0 + (unsigned)((C % XB) * XSLAB + 8 * J * 128);
    if constexpr (J < 4) {
        asm volatile("s_mov_b32 m0, %0\n\ts_nop 0\n\tbuffer_load_dwordx4 %1, %2, %3 offen lds"
                     :: "s"(m0), "v"(c.xvoff[J]), "s"(c.xsrd), "s"((unsigned)(C * 128)) : "m0", "memory");
    } else {
        unsigned long long keep;
        asm volatile("s_mov_b32 m0, %1\n\ts_mov_b64 %0, exec\n\ts_mov_b64 exec, 0xffff\n\t"
                     "buffer_load_dwordx4 %2, %3, %4 offen lds\n\ts_mov_b64 exec, %0"
                     : "=&s"(keep) : "s"(m0), "v"(c.xvoff[4]), "s"(c.xsrd), "s"((unsigned)(C * 128)) : "m0", "memory");
    }
}
template <int S> DI void dma_w(Ctx& c) {
    const unsigned m0 = c.wm0 + (unsigned)((S % RS) * WSLOT);
    constexpr unsigned soff = (unsigned)((S % 5) * (512 * 2048) + (S / 5) * (64 * 2048));
    asm volatile("s_mov_b32 m0, %0\n\ts_nop 0\n\t"
                 "buffer_load_dwordx4 %1, %2, %3 offen lds\n\t"
                 "buffer_load_dwordx4 %1, %2, %3 offen offset:1024 lds"
                 :: "s"(m0), "v"(c.wvoff), "s"(c.wsrd), "s"(soff) : "m0", "memory");
}
template <int P> DI void pro_dma(Ctx& c) {
    if constexpr (PRO_KIND[P] == 0) {
        dma_x<PRO_IDX[P], 0>(c); dma_x<PRO_IDX[P], 1>(c); dma_x<PRO_IDX[P], 2>(c); dma_x<PRO_IDX[P], 3>(c); dma_x<PRO_IDX[P], 4>(c);
    } else dma_w<PRO_IDX[P]>(c);
}

template <int S, int MI> DI void rd_a(Ctx& c) {
    asm volatile("ds_read_b128 %0, %1 offset:%2" : "=v"(c.A[S & 1][MI])
                 : "v"(c.aaddr[S % 5]), "i"(LDS_X + ((S / 5) % XB) * XSLAB + MI * 4096) : "memory");
}
template <int S, int FG, int TH> DI void rd_b(Ctx& c) {
    asm volatile("ds_read_b64_tr_b16 %0, %1 offset:%2" : "=v"(c.Bh[S & 1][FG][TH])
                 : "v"(c.baddr), "i"((S % RS) * WSLOT + FG * 1024 + TH * 256) : "memory");
}
template <int S> DI void wait_frags(Ctx& c) {
    asm volatile("s_waitcnt lgkmcnt(0)"
                 : "+v"(c.A[S & 1][0]), "+v"(c.A[S & 1][1]), "+v"(c.A[S & 1][2]), "+v"(c.A[S & 1][3]),
                   "+v"(c.Bh[S & 1][0][0]), "+v"(c.Bh[S & 1][0][1]), "+v"(c.Bh[S & 1][1][0]), "+v"(c.Bh[S & 1][1][1])
                 :: "memory");
}
template <int S> DI void rd_all(Ctx& c) {
    rd_a<S, 0>(c); rd_b<S, 0, 0>(c); rd_b<S, 0, 1>(c); rd_a<S, 1>(c); rd_b<S, 1, 0>(c); rd_b<S, 1, 1>(c); rd_a<S, 2>(c); rd_a<S, 3>(c);
}
template <int S, int MI, int FG> DI void mma(Ctx& c) {
    const bf16x8 b = __builtin_bit_cast(bf16x8, __builtin_shufflevector(c.Bh[S & 1][FG][0], c.Bh[S & 1][FG][1], 0, 1, 2, 3));
    c.acc[MI][FG] = __builtin_amdgcn_mfma_f32_32x32x16_bf16(b, c.A[S & 1][MI], c.acc[MI][FG], 0, 0, 0);
}

template <int U> DI void step(Ctx& c) {
    constexpr bool NX = (U + 1 < NSTEP);
    if constexpr (NX) {
        if constexpr (U % 5 == 4) asm volatile("s_waitcnt vmcnt(%0)\n\ts_barrier" :: "i"(SCHED.wait_n[U]) : "memory");
        else                      asm volatile("s_waitcnt vmcnt(%0)" :: "i"(SCHED.wait_n[U]) : "memory");
    }
    GAP();
    mma<U, 0, 0>(c); if constexpr (NX) { rd_a<U + 1, 0>(c); } GAP();
    mma<U, 0, 1>(c); if constexpr (NX) { rd_b<U + 1, 0, 0>(c); rd_b<U + 1, 0, 1>(c); } GAP();
    mma<U, 1, 0>(c); if constexpr (NX) { rd_a<U + 1, 1>(c); } GAP();
    mma<U, 1, 1>(c); if constexpr (NX) { rd_b<U + 1, 1, 0>(c); rd_b<U + 1, 1, 1>(c); } GAP();
    mma<U, 2, 0>(c); if constexpr (NX) { rd_a<U + 1, 2>(c); } GAP();
    mma<U, 2, 1>(c); if constexpr (NX) { rd_a<U + 1, 3>(c); } GAP();
    mma<U, 3, 0>(c); if constexpr (U + RS < NSTEP) { dma_w<U + RS>(c); } GAP();
    mma<U, 3, 1>(c); if constexpr (U >= 4 && (U - 4) / 5 + XB < 8) { dma_x<(U - 4) / 5 + XB, (U - 4) % 5>(c); } GAP();
    if constexpr (NX) wait_frags<U + 1>(c);
    GAP();
}
template <int... Us> DI void run_steps(Ctx& c, std::integer_sequence<int, Us...>) { (step<Us>(c), ...); }
template <int... Ps> DI void run_pro(Ctx& c, std::integer_sequence<int, Ps...>) { (pro_dma<Ps>(c), ...); }

DI float gate(float f, float g) {
    const float fc = fminf(fmaxf(f, -30.f), 30.f);
    const float e2f = __builtin_amdgcn_exp2f(fc * 2.8853900817779268f);
    const float eg  = __builtin_amdgcn_exp2f(g * -1.4426950408889634f);
    return (e2f - 1.f) * __builtin_amdgcn_rcpf((e2f + 1.f) * (1.f + eg));
}
DI unsigned pack_bf16(float lo, float hi) {
    typedef float f32x2 __attribute__((ext_vector_type(2)));
    typedef __bf16 bf16x2 __attribute__((ext_vector_type(2)));
    f32x2 v = {lo, hi};
    return __builtin_bit_cast(unsigned, __builtin_convertvector(v, bf16x2));
}

__global__ __launch_bounds__(256, 1)
void conv_gate_kernel(const __hip_bfloat16* __restrict__ x, const __hip_bfloat16* __restrict__ wt,
                      __hip_bfloat16* __restrict__ out) {
    __shared__ __attribute__((aligned(16))) char lds[LDS_B];
    const unsigned ldsb = (unsigned)(size_t)lds;

    const int tid = threadIdx.x, lane = tid & 63;
    const int w = __builtin_amdgcn_readfirstlane(tid >> 6);
    const int h = lane >> 5;

    const int bid = blockIdx.x, xcd = bid & 7, jj = bid >> 3;
    const int nt = (xcd & 3) * 4 + (jj & 3);
    const int mt = (xcd >> 2) * 8 + (jj >> 2);
    const int batch = mt >> 3, t0 = (mt & 7) * 128, n0 = nt * 32;

    Ctx c;
    c.xsrd = __builtin_amdgcn_make_buffer_rsrc((void*)((const char*)x + (size_t)batch * TLEN * 1024), (short)0, TLEN * 1024, 0x00020000);
    c.wsrd = __builtin_amdgcn_make_buffer_rsrc((void*)wt, (short)0, 5 * 512 * 2048, 0x00020000);
    c.xm0 = ldsb + LDS_X + (unsigned)(34 * 128) * (unsigned)w;
    c.wm0 = ldsb + LDS_W + (unsigned)WRING * (unsigned)w;
#pragma unroll
    for (int i = 0; i < 5; ++i) {
        const int r = (lane & 31) + 2 * i;
        c.aaddr[i] = ldsb + LDS_X + r * 128 + (((2 * w + h) ^ ((r >> 1) & 7)) << 4);
    }
    c.baddr = ldsb + LDS_W + w * WRING + (8 * h + ((lane & 15) >> 2)) * 64 + ((lane >> 4) & 1) * 32 + (lane & 3) * 8;
#pragma unroll
    for (int j = 0; j < 5; ++j) {
        const int r = 34 * w + 8 * j + (lane >> 3);
        const int trow = t0 - 8 + r;
        const int src = (lane & 7) ^ ((r >> 1) & 7);
        c.xvoff[j] = trow < 0 ? 0x80000000u : (unsigned)(trow * 1024 + src * 16);
    }
    c.wvoff = (unsigned)((16 * w + (lane >> 2)) * 2048 + n0 * 2 + (lane & 3) * 16);

    run_pro(c, std::make_integer_sequence<int, NPRO>{});
    asm volatile("s_waitcnt vmcnt(%0)\n\ts_barrier" :: "i"(SCHED.pro_n) : "memory");
    GAP();
    rd_all<0>(c);
#pragma unroll
    for (int mi = 0; mi < 4; ++mi)
#pragma unroll
        for (int fg = 0; fg < 2; ++fg)
#pragma unroll
            for (int e = 0; e < 16; ++e) c.acc[mi][fg][e] = 0.f;
    wait_frags<0>(c);
    GAP();

    run_steps(c, std::make_integer_sequence<int, NSTEP>{});

    __syncthreads();
    {
        f32x4* red = (f32x4*)lds;
#pragma unroll
        for (int mi = 0; mi < 4; ++mi)
#pragma unroll
            for (int fg = 0; fg < 2; ++fg)
#pragma unroll
                for (int rg = 0; rg < 4; ++rg) {
                    f32x4 v = {c.acc[mi][fg][4 * rg], c.acc[mi][fg][4 * rg + 1], c.acc[mi][fg][4 * rg + 2], c.acc[mi][fg][4 * rg + 3]};
                    red[(((w * 4 + mi) * 2 + fg) * 4 + rg) * 64 + lane] = v;
                }
    }
    __syncthreads();
    {
        const f32x4* red = (const f32x4*)lds;
        const int trow = t0 + 32 * w + (lane & 31);
        __hip_bfloat16* orow = out + ((size_t)batch * TLEN + trow) * 512 + n0 + 4 * h;
#pragma unroll
        for (int rg = 0; rg < 4; ++rg) {
            f32x4 f = red[(((0 * 4 + w) * 2 + 0) * 4 + rg) * 64 + lane];
            f32x4 g = red[(((0 * 4 + w) * 2 + 1) * 4 + rg) * 64 + lane];
#pragma unroll
            for (int s = 1; s < 4; ++s) {
                f += red[(((s * 4 + w) * 2 + 0) * 4 + rg) * 64 + lane];
                g += red[(((s * 4 + w) * 2 + 1) * 4 + rg) * 64 + lane];
            }
            u32x2 o;
            o[0] = pack_bf16(gate(f[0], g[0]), gate(f[1], g[1]));
            o[1] = pack_bf16(gate(f[2], g[2]), gate(f[3], g[3]));
            if (trow < TLEN) *(u32x2*)(orow + 8 * rg) = o;
        }
    }
}

}

extern "C" void kernel_launch(void* const* d_in, const int* in_sizes, int n_in,
                              void* d_out, int out_size, void* d_ws, size_t ws_size,
                              hipStream_t stream) {
    const __hip_bfloat16* x  = (const __hip_bfloat16*)d_in[0];
    const __hip_bfloat16* wt = (const __hip_bfloat16*)d_in[1];
    __hip_bfloat16* out = (__hip_bfloat16*)d_out;
    conv_gate_kernel<<<256, 256, 0, stream>>>(x, wt, out);
}
```

```cpp
#include <hip/hip_runtime.h>
#include <hip/hip_bf16.h>
#include <stdint.h>
#include <utility>

typedef float    f32x16 __attribute__((ext_vector_type(16)));
typedef float    f32x4  __attribute__((ext_vector_type(4)));
typedef __bf16   bf16x8 __attribute__((ext_vector_type(8)));
typedef unsigned u32x2  __attribute__((ext_vector_type(2)));
typedef unsigned u32x4  __attribute__((ext_vector_type(4)));

#define DI __device__ __forceinline__
#define GAP() __builtin_amdgcn_sched_barrier(0)

namespace {

constexpr int TLEN   = 1000;
constexpr int NSTEP  = 40;
constexpr int XB     = 4;
constexpr int RS     = 10;
constexpr int XROWS  = 136;
constexpr int XSLAB  = XROWS * 128;
constexpr int WSLOT  = 2048;
constexpr int WRING  = RS * WSLOT;
constexpr int LDS_X  = 0;
constexpr int LDS_W  = XB * XSLAB;
constexpr int LDS_F  = LDS_W + 4 * WRING;
constexpr int LDS_B  = LDS_F + 256;
constexpr int RED_B  = 4 * 4 * 2 * 4096;
static_assert(RED_B <= LDS_B, "reduction image must fit");

constexpr int PRO_INSTR = 40, NSLOT = 4;
constexpr int TOK_RATE = 65, TOK_CAP = 130, TOK_W = 52, TOK_X = 16;
struct Plan {
    int pro[40]; int npro;
    int stp[NSTEP][NSLOT];
    int pro_n; int wait_n[NSTEP];
    int xsig[NSTEP];
    bool ok;
    constexpr Plan() : pro(), npro(0), stp(), pro_n(0), wait_n(), xsig(), ok(true) {
        int q[80] = {}; int nq = 0;
        for (int s = 0; s < NSTEP; ++s) {
            if (s % 5 == 0) for (int j = 0; j < 5; ++j) q[nq++] = 100 + (s / 5) * 5 + j;
            q[nq++] = s;
        }
        bool done[80] = {};
        int pos = 0; int endpos[140] = {};
        auto ready = [](int op) { return op < 100 ? (op < RS ? -1 : op - RS) : ((op - 100) / 5 < XB ? -1 : 5 * ((op - 100) / 5 - XB) + 4); };
        auto cost  = [](int op) { return op < 100 ? 2 : 1; };
        int cnt = 0, tok = 0;
        for (int i = 0; i < nq; ++i) {
            if (ready(q[i]) >= 0 || cnt + cost(q[i]) > PRO_INSTR) break;
            pro[npro++] = q[i]; done[i] = true; cnt += cost(q[i]); pos += cost(q[i]); endpos[q[i]] = pos;
        }
        { int need = endpos[0]; for (int j = 0; j < 5; ++j) if (endpos[100 + j] > need) need = endpos[100 + j]; pro_n = pos - need; }
        for (int u = 0; u < NSTEP; ++u) {
            int need = 0;
            xsig[u] = -1;
            if (u + 1 < NSTEP) {
                if (endpos[u + 1] == 0) ok = false;
                need = endpos[u + 1];
                if ((u + 3) % 5 == 0 && (u + 3) / 5 < 8) {
                    const int cc = (u + 3) / 5; xsig[u] = cc;
                    for (int j = 0; j < 5; ++j) { int e = endpos[100 + 5 * cc + j]; if (e == 0) ok = false; if (e > need) need = e; }
                }
            }
            int n = pos - need; wait_n[u] = n > 63 ? 63 : n;
            tok += TOK_RATE; if (tok > TOK_CAP) tok = TOK_CAP;
            int k = 0;
            for (int j = 0; j < NSLOT; ++j) stp[u][j] = -1;
            for (int i = 0; i < nq && k < NSLOT; ++i) {
                if (done[i] || ready(q[i]) > u) continue;
                const int tc = q[i] < 100 ? TOK_W : TOK_X;
                if (tc > tok) break;
                stp[u][k++] = q[i]; done[i] = true; tok -= tc; pos += cost(q[i]); endpos[q[i]] = pos;
            }
        }
        for (int i = 0; i < nq; ++i) if (!done[i]) ok = false;
    }
};
constexpr Plan PLAN{};
static_assert(PLAN.ok, "DMA plan misses a deadline");

struct Ctx {
    f32x16 acc[4][2];
    bf16x8 A[2][4];
    u32x2  Bh[2][2][2];
    unsigned aaddr[5];
    unsigned baddr;
    unsigned xvoff[5];
    unsigned wvoff;
    u32x4 fpre;
    unsigned faddr;
    int w;
    unsigned xm0, wm0;
    unsigned csoff[8];
    __amdgpu_buffer_rsrc_t xsrd, wsrd;
};

DI void flag_set(Ctx& c, int idx) {
    asm volatile("ds_write_b32 %0, %1" :: "v"(c.faddr + (unsigned)(idx + c.w) * 4u), "v"(1u) : "memory");
}
DI void flag_wait(Ctx& c, int idx) {
    for (int spin = 0; spin < (1 << 20); ++spin) {
        u32x4 v;
        asm volatile("ds_read_b128 %0, %1\n\ts_waitcnt lgkmcnt(0)" : "=v"(v) : "v"(c.faddr + (unsigned)idx * 4u) : "memory");
        if (__builtin_amdgcn_readfirstlane(v[0] & v[1] & v[2] & v[3])) break;
    }
}

DI void flag_prefetch(Ctx& c, int idx) {
    asm volatile("ds_read_b128 %0, %1" : "=v"(c.fpre) : "v"(c.faddr + (unsigned)idx * 4u) : "memory");
}
DI void flag_check(Ctx& c, int idx) {
    if (!__builtin_amdgcn_readfirstlane(c.fpre[0] & c.fpre[1] & c.fpre[2] & c.fpre[3])) flag_wait(c, idx);
}

template <int C, int J> DI void dma_x(Ctx& c) {
    const unsigned m0 = c.xm0 + (unsigned)((C % XB) * XSLAB + 8 * J * 128);
    if constexpr (J < 4) {
        asm volatile("s_mov_b32 m0, %0\n\ts_nop 0\n\tbuffer_load_dwordx4 %1, %2, %3 offen lds"
                     :: "s"(m0), "v"(c.xvoff[J]), "s"(c.xsrd), "s"(c.csoff[C] >> 10) : "m0", "memory");
    } else {
        unsigned long long keep;
        asm volatile("s_mov_b32 m0, %1\n\ts_mov_b64 %0, exec\n\ts_mov_b64 exec, 0xffff\n\t"
                     "buffer_load_dwordx4 %2, %3, %4 offen lds\n\ts_mov_b64 exec, %0"
                     : "=&s"(keep) : "s"(m0), "v"(c.xvoff[4]), "s"(c.xsrd), "s"(c.csoff[C] >> 10) : "m0", "memory");
    }
}
template <int S> DI void dma_w(Ctx& c) {
    const unsigned m0 = c.wm0 + (unsigned)((S % RS) * WSLOT);
    const unsigned soff = c.csoff[S / 5] + (unsigned)((S % 5) * (512 * 2048));
    asm volatile("s_mov_b32 m0, %0\n\ts_nop 0\n\t"
                 "buffer_load_dwordx4 %1, %2, %3 offen lds\n\t"
                 "buffer_load_dwordx4 %1, %2, %3 offen offset:1024 lds"
                 :: "s"(m0), "v"(c.wvoff), "s"(c.wsrd), "s"(soff) : "m0", "memory");
}
template <int OP> DI void dma_op(Ctx& c) {
    if constexpr (OP < 0) {}
    else if constexpr (OP < 100) dma_w<OP>(c);
    else {
        if constexpr ((OP - 100) % 5 == 0 && (OP - 100) / 5 >= XB) flag_wait(c, 32 + 4 * ((OP - 100) / 5 - XB));
        dma_x<(OP - 100) / 5, (OP - 100) % 5>(c);
    }
}

template <int S, int MI> DI void rd_a(Ctx& c) {
    asm volatile("ds_read_b128 %0, %1 offset:%2" : "=v"(c.A[S & 1][MI])
                 : "v"(c.aaddr[S % 5]), "i"(LDS_X + ((S / 5) % XB) * XSLAB + MI * 4096) : "memory");
}
template <int S, int FG, int TH> DI void rd_b(Ctx& c) {
    asm volatile("ds_read_b64_tr_b16 %0, %1 offset:%2" : "=v"(c.Bh[S & 1][FG][TH])
                 : "v"(c.baddr), "i"((S % RS) * WSLOT + FG * 1024 + TH * 256) : "memory");
}
template <int S> DI void wait_frags(Ctx& c) {
    asm volatile("s_waitcnt lgkmcnt(0)"
                 : "+v"(c.A[S & 1][0]), "+v"(c.A[S & 1][1]), "+v"(c.A[S & 1][2]), "+v"(c.A[S & 1][3]),
                   "+v"(c.Bh[S & 1][0][0]), "+v"(c.Bh[S & 1][0][1]), "+v"(c.Bh[S & 1][1][0]), "+v"(c.Bh[S & 1][1][1]), "+v"(c.fpre)
                 :: "memory");
}
template <int S> DI void rd_all(Ctx& c) {
    rd_a<S, 0>(c); rd_b<S, 0, 0>(c); rd_b<S, 0, 1>(c); rd_a<S, 1>(c); rd_b<S, 1, 0>(c); rd_b<S, 1, 1>(c); rd_a<S, 2>(c); rd_a<S, 3>(c);
}
template <int S, int MI, int FG> DI void mma(Ctx& c) {
    const bf16x8 b = __builtin_bit_cast(bf16x8, __builtin_shufflevector(c.Bh[S & 1][FG][0], c.Bh[S & 1][FG][1], 0, 1, 2, 3));
    c.acc[MI][FG] = __builtin_amdgcn_mfma_f32_32x32x16_bf16(b, c.A[S & 1][MI], c.acc[MI][FG], 0, 0, 0);
}

template <int U> DI void step(Ctx& c) {
    constexpr bool NX = (U + 1 < NSTEP);
    if constexpr (NX) {
        asm volatile("s_waitcnt vmcnt(%0)" :: "i"(PLAN.wait_n[U]) : "memory");
        if constexpr (PLAN.xsig[U] >= 0) flag_set(c, 4 * PLAN.xsig[U]);
        if constexpr (U % 5 == 4) { flag_set(c, 32 + 4 * (U / 5)); flag_check(c, 4 * ((U + 1) / 5)); }
    }
    GAP();
    mma<U, 0, 0>(c); if constexpr (NX) { rd_a<U + 1, 0>(c); } if constexpr (U % 5 == 3 && U + 2 < NSTEP) flag_prefetch(c, 4 * ((U + 2) / 5)); GAP();
    mma<U, 0, 1>(c); if constexpr (NX) { rd_b<U + 1, 0, 0>(c); rd_b<U + 1, 0, 1>(c); } GAP();
    mma<U, 1, 0>(c); if constexpr (NX) { rd_a<U + 1, 1>(c); } GAP();
    mma<U, 1, 1>(c); if constexpr (NX) { rd_b<U + 1, 1, 0>(c); rd_b<U + 1, 1, 1>(c); } GAP();
    mma<U, 2, 0>(c); if constexpr (NX) { rd_a<U + 1, 2>(c); } dma_op<PLAN.stp[U][0]>(c); GAP();
    mma<U, 2, 1>(c); if constexpr (NX) { rd_a<U + 1, 3>(c); } dma_op<PLAN.stp[U][1]>(c); GAP();
    mma<U, 3, 0>(c); dma_op<PLAN.stp[U][2]>(c); GAP();
    mma<U, 3, 1>(c); dma_op<PLAN.stp[U][3]>(c); GAP();
    if constexpr (NX) wait_frags<U + 1>(c);
    GAP();
}
template <int... Us> DI void run_steps(Ctx& c, std::integer_sequence<int, Us...>) { (step<Us>(c), ...); }
template <int... Ps> DI void run_pro(Ctx& c, std::integer_sequence<int, Ps...>) { (dma_op<PLAN.pro[Ps]>(c), ...); }

DI float gate(float f, float g) {
    const float fc = fminf(fmaxf(f, -30.f), 30.f);
    const float e2f = __builtin_amdgcn_exp2f(fc * 2.8853900817779268f);
    const float eg  = __builtin_amdgcn_exp2f(g * -1.4426950408889634f);
    return (e2f - 1.f) * __builtin_amdgcn_rcpf((e2f + 1.f) * (1.f + eg));
}
DI unsigned pack_bf16(float lo, float hi) {
    typedef float f32x2 __attribute__((ext_vector_type(2)));
    typedef __bf16 bf16x2 __attribute__((ext_vector_type(2)));
    f32x2 v = {lo, hi};
    return __builtin_bit_cast(unsigned, __builtin_convertvector(v, bf16x2));
}

__global__ __launch_bounds__(256, 1)
void conv_gate_kernel(const __hip_bfloat16* __restrict__ x, const __hip_bfloat16* __restrict__ wt,
                      __hip_bfloat16* __restrict__ out) {
    __shared__ __attribute__((aligned(16))) char lds[LDS_B];
    const unsigned ldsb = (unsigned)(size_t)lds;

    const int tid = threadIdx.x, lane = tid & 63;
    const int w = __builtin_amdgcn_readfirstlane(tid >> 6);
    const int h = lane >> 5;

    const int bid = blockIdx.x, xcd = bid & 7, jj = bid >> 3;
    const int nt = (xcd & 3) * 4 + (jj & 3);
    const int mt = (xcd >> 2) * 8 + (jj >> 2);
    const int batch = mt >> 3, t0 = (mt & 7) * 128, n0 = nt * 32;

    Ctx c;
    c.fpre = (u32x4){0u, 0u, 0u, 0u};
    c.xsrd = __builtin_amdgcn_make_buffer_rsrc((void*)((const char*)x + (size_t)batch * TLEN * 1024), (short)0, TLEN * 1024, 0x00020000);
    c.wsrd = __builtin_amdgcn_make_buffer_rsrc((void*)wt, (short)0, 5 * 512 * 2048, 0x00020000);
    c.w = w;
    c.faddr = ldsb + LDS_F;
    if (tid < 64) *(unsigned*)(lds + LDS_F + tid * 4) = 0u;
    __syncthreads();
    c.xm0 = ldsb + LDS_X + (unsigned)(34 * 128) * (unsigned)w;
    c.wm0 = ldsb + LDS_W + (unsigned)WRING * (unsigned)w;
#pragma unroll
    for (int i = 0; i < 5; ++i) {
        const int r = (lane & 31) + 2 * i;
        c.aaddr[i] = ldsb + LDS_X + r * 128 + (((2 * w + h) ^ ((r >> 1) & 7)) << 4);
    }
    c.baddr = ldsb + LDS_W + w * WRING + (8 * h + ((lane & 15) >> 2)) * 64 + ((lane >> 4) & 1) * 32 + (lane & 3) * 8;
#pragma unroll
    for (int j = 0; j < 5; ++j) {
        const int r = 34 * w + 8 * j + (lane >> 3);
        const int trow = t0 - 8 + r;
        const int src = (lane & 7) ^ ((r >> 1) & 7);
        c.xvoff[j] = trow < 0 ? 0x80000000u : (unsigned)(trow * 1024 + src * 16);
    }
    {
        const int c0 = ((jj >> 2) + 2 * (jj & 3)) & 7;
#pragma unroll
        for (int j = 0; j < 8; ++j) c.csoff[j] = (unsigned)(((j + c0) & 7) * (64 * 2048));
    }
    c.wvoff = (unsigned)((16 * w + (lane >> 2)) * 2048 + n0 * 2 + (lane & 3) * 16);

    run_pro(c, std::make_integer_sequence<int, PLAN.npro>{});
    asm volatile("s_waitcnt vmcnt(%0)" :: "i"(PLAN.pro_n) : "memory");
    flag_set(c, 0);
    flag_wait(c, 0);
    GAP();
    rd_all<0>(c);
#pragma unroll
    for (int mi = 0; mi < 4; ++mi)
#pragma unroll
        for (int fg = 0; fg < 2; ++fg)
#pragma unroll
            for (int e = 0; e < 16; ++e) c.acc[mi][fg][e] = 0.f;
    wait_frags<0>(c);
    GAP();

    run_steps(c, std::make_integer_sequence<int, NSTEP>{});

    __syncthreads();
    {
        f32x4* red = (f32x4*)lds;
        auto put = [&](auto MI) {
            constexpr int mi = decltype(MI)::value;
            if (w != mi) {
#pragma unroll
                for (int fg = 0; fg < 2; ++fg)
#pragma unroll
                    for (int rg = 0; rg < 4; ++rg) {
                        f32x4 v = {c.acc[mi][fg][4 * rg], c.acc[mi][fg][4 * rg + 1], c.acc[mi][fg][4 * rg + 2], c.acc[mi][fg][4 * rg + 3]};
                        red[(((w * 4 + mi) * 2 + fg) * 4 + rg) * 64 + lane] = v;
                    }
            }
        };
        put(std::integral_constant<int, 0>{}); put(std::integral_constant<int, 1>{});
        put(std::integral_constant<int, 2>{}); put(std::integral_constant<int, 3>{});
    }
    f32x16 of, og;
    if (w == 0)      { of = c.acc[0][0]; og = c.acc[0][1]; }
    else if (w == 1) { of = c.acc[1][0]; og = c.acc[1][1]; }
    else if (w == 2) { of = c.acc[2][0]; og = c.acc[2][1]; }
    else             { of = c.acc[3][0]; og = c.acc[3][1]; }
    __syncthreads();
    {
        const f32x4* red = (const f32x4*)lds;
        const int trow = t0 + 32 * w + (lane & 31);
        __hip_bfloat16* orow = out + ((size_t)batch * TLEN + trow) * 512 + n0 + 4 * h;
        const int s1 = (w + 1) & 3, s2 = (w + 2) & 3, s3 = (w + 3) & 3;
#pragma unroll
        for (int rg = 0; rg < 4; ++rg) {
            f32x4 f = {of[4 * rg], of[4 * rg + 1], of[4 * rg + 2], of[4 * rg + 3]};
            f32x4 g = {og[4 * rg], og[4 * rg + 1], og[4 * rg + 2], og[4 * rg + 3]};
            f += red[(((s1 * 4 + w) * 2 + 0) * 4 + rg) * 64 + lane]; g += red[(((s1 * 4 + w) * 2 + 1) * 4 + rg) * 64 + lane];
            f += red[(((s2 * 4 + w) * 2 + 0) * 4 + rg) * 64 + lane]; g += red[(((s2 * 4 + w) * 2 + 1) * 4 + rg) * 64 + lane];
            f += red[(((s3 * 4 + w) * 2 + 0) * 4 + rg) * 64 + lane]; g += red[(((s3 * 4 + w) * 2 + 1) * 4 + rg) * 64 + lane];
            u32x2 o;
            o[0] = pack_bf16(gate(f[0], g[0]), gate(f[1], g[1]));
            o[1] = pack_bf16(gate(f[2], g[2]), gate(f[3], g[3]));
            if (trow < TLEN) *(u32x2*)(orow + 8 * rg) = o;
        }
    }
}

}

extern "C" void kernel_launch(void* const* d_in, const int* in_sizes, int n_in,
                              void* d_out, int out_size, void* d_ws, size_t ws_size,
                              hipStream_t stream) {
    const __hip_bfloat16* x  = (const __hip_bfloat16*)d_in[0];
    const __hip_bfloat16* wt = (const __hip_bfloat16*)d_in[1];
    __hip_bfloat16* out = (__hip_bfloat16*)d_out;
    conv_gate_kernel<<<256, 256, 0, stream>>>(x, wt, out);
}
```
